```python
import torch

def _softmax(x, axis=-1):
    return torch.softmax(x, dim=axis)
def _gelu(x):
    return torch.nn.functional.gelu(x, approximate="tanh")
def _silu(x):
    return torch.nn.functional.silu(x)
def _sigmoid(x):
    return torch.sigmoid(x)
def _layernorm(x, axis=-1, eps=1e-5):
    m = x.mean(dim=axis, keepdim=True)
    v = x.var(dim=axis, keepdim=True, unbiased=False)
    return (x - m) / torch.sqrt(v + eps)
def _scatter_add(tgt, idx, src, axis=0):
    return tgt.clone().index_add_(axis, idx, src)
def _segment_sum(src, seg, n):
    out = torch.zeros((n,) + src.shape[1:], dtype=src.dtype)
    return out.index_add_(0, seg, src)
def _pad(x, pw):
    flat = tuple(v for pair in reversed(pw) for v in pair)
    return torch.nn.functional.pad(x, flat)

kernel_name = 'standalone__paged_block_table_kv_radixattention__decode'

def setup_inputs():

    _w = (lambda a: (lambda t: t.to(torch.bfloat16) if t.is_floating_point() else t)(torch.as_tensor(a)))
    B, H, Hkv, Dh = 4, 32, 32, 128
    Lk = 8192
    bs = 16
    nb = max(2, Lk // bs)
    P = max(1, nb // 2)
    n_phys = P + B * (nb - P)
    shared = torch.arange(P, dtype=torch.int64)
    priv = P + torch.arange(B * (nb - P), dtype=torch.int64).reshape(B, nb - P)
    bt = torch.cat([torch.tile(shared[None, :], (B, 1)), priv], dim=1)
    ctx = nb * bs - (torch.arange(1, B + 1, dtype=torch.int64) * 2654435761) % max(1, (nb * bs - P * bs) // 2)
    return {
        "q": _w((lambda *s: torch.randn(*s, dtype=torch.bfloat16) * 0.8)(B, H, Dh)),
        "k_cache": _w((lambda *s: torch.randn(*s, dtype=torch.bfloat16) * 0.8)(n_phys, bs, Hkv, Dh)),
        "v_cache": _w((lambda *s: torch.randn(*s, dtype=torch.bfloat16) * 0.05)(n_phys, bs, Hkv, Dh)),
        "block_table": _w(bt.to(torch.int64)),
        "ctx_lens": _w(torch.as_tensor(ctx).to(torch.int64)),
    }

def reference(q, k_cache, v_cache, block_table, ctx_lens):
    B, H, Dh = q.shape
    n_phys, bs, Hkv, _ = k_cache.shape
    nb = block_table.shape[1]
    Lk = nb * bs
    rep = H // Hkv
    k = k_cache[block_table].reshape(B, Lk, Hkv, Dh)
    v = v_cache[block_table].reshape(B, Lk, Hkv, Dh)
    k = (lambda x, n, axis: torch.repeat_interleave(x, n, dim=axis))(k, rep, axis=2)
    v = (lambda x, n, axis: torch.repeat_interleave(x, n, dim=axis))(v, rep, axis=2)
    scores = torch.einsum("bhd,bkhd->bhk", q, k) * (Dh ** -0.5)
    pos = torch.arange(Lk)
    valid = pos[None, :] < ctx_lens[:, None]
    scores = torch.where(valid[:, None, :], scores, -1e9)
    p = _softmax(scores, axis=-1)
    return torch.einsum("bhk,bkhd->bhd", p, v)
```

```cpp
#include <hip/hip_runtime.h>
#include <hip/hip_bf16.h>
#include <cstdint>

#define XW 8
#define XW2 4
#define EXTRA -6
#define SLEEP 4
#define NEARN 1
#define ABL 0

namespace {
constexpr int B = 4, H = 32, D = 128, PG = 16, NB = 512, LK = NB * PG;
constexpr int NSPLIT = 8, NW = 8, NTHR = 2 * NW * 64, NSLOT = 2 * NSPLIT;
constexpr unsigned PAGE_BYTES = PG * H * D * 2, TOK_BYTES = H * D * 2, HEAD_BYTES = D * 2;
constexpr float C2 = 0.088388347648318440f * 1.4426950408889634f;
constexpr float M_INIT = -1e30f;
constexpr int NSTAT = 2 * NSPLIT * NW;
constexpr int MAXU = (B * NB) / NSPLIT + 16;
constexpr int REC = 132;
constexpr int CONS = NSPLIT - 1;
constexpr unsigned WS_LINE = 128, WS_REC = 65536, GREC_BYTES = 512;
constexpr unsigned WS_BYTES = WS_REC + H * B * NSLOT * GREC_BYTES;
static_assert(NW * 64 == NB && NSTAT == 128 && NW >= B, "one planning thread per logical page; planning wave b finishes sequence b");

typedef unsigned u32x4 __attribute__((ext_vector_type(4)));
typedef unsigned u32x2 __attribute__((ext_vector_type(2)));
typedef float f32x2 __attribute__((ext_vector_type(2)));
typedef float f32x4 __attribute__((ext_vector_type(4)));
typedef __bf16 bf16x2 __attribute__((ext_vector_type(2)));
typedef __bf16 bf16x8 __attribute__((ext_vector_type(8)));
typedef short s16x4 __attribute__((ext_vector_type(4)));
typedef unsigned long long u64;
typedef const __attribute__((address_space(4))) int cint;

template <int CTRL> __device__ __forceinline__ float dpp(float x) {
  return __builtin_bit_cast(float, __builtin_amdgcn_mov_dpp(__builtin_bit_cast(int, x), CTRL, 0xf, 0xf, true));
}
template <int CTRL> __device__ __forceinline__ unsigned dppu(unsigned x) { return (unsigned)__builtin_amdgcn_mov_dpp((int)x, CTRL, 0xf, 0xf, true); }
constexpr int XOR1 = 0xB1, XOR2 = 0x4E, XOR7 = 0x141, BCAST = 0x150, Q0 = 0x00, Q1 = 0x55, Q2 = 0xAA, Q3 = 0xFF;
__device__ __forceinline__ float ex2(float x) { return __builtin_amdgcn_exp2f(x); }
__device__ __forceinline__ float xrow_max(float x) {
  auto s = __builtin_amdgcn_permlane16_swap(__float_as_uint(x), __float_as_uint(x), false, false);
  x = fmaxf(__uint_as_float(s[0]), __uint_as_float(s[1]));
  auto t = __builtin_amdgcn_permlane32_swap(__float_as_uint(x), __float_as_uint(x), false, false);
  return fmaxf(__uint_as_float(t[0]), __uint_as_float(t[1]));
}
__device__ __forceinline__ float xrow_sum(float x) {
  auto s = __builtin_amdgcn_permlane16_swap(__float_as_uint(x), __float_as_uint(x), false, false);
  x = __uint_as_float(s[0]) + __uint_as_float(s[1]);
  auto t = __builtin_amdgcn_permlane32_swap(__float_as_uint(x), __float_as_uint(x), false, false);
  return __uint_as_float(t[0]) + __uint_as_float(t[1]);
}
__device__ __forceinline__ float rdl(float x, int l) { return __builtin_bit_cast(float, __builtin_amdgcn_readlane(__builtin_bit_cast(int, x), l)); }
__device__ __forceinline__ float rfl(float x) { return __builtin_bit_cast(float, __builtin_amdgcn_readfirstlane(__builtin_bit_cast(int, x))); }
__device__ __forceinline__ int mbcnt(u64 m) { return (int)__builtin_amdgcn_mbcnt_hi((unsigned)(m >> 32), __builtin_amdgcn_mbcnt_lo((unsigned)m, 0u)); }

struct Part { f32x2 o; float M, L; };

__device__ __forceinline__ Part merge_waves(const float* rb, int lane) {
  const f32x2 ml = *reinterpret_cast<const f32x2*>(rb + (lane & 7) * B * REC);
  f32x2 a[NW];
#pragma unroll
  for (int k = 0; k < NW; ++k) a[k] = *reinterpret_cast<const f32x2*>(rb + k * B * REC + 4 + 2 * lane);
  Part p;
  float M = ml[0];
  M = fmaxf(M, dpp<XOR1>(M)); M = fmaxf(M, dpp<XOR2>(M)); M = fmaxf(M, dpp<XOR7>(M));
  const float w = ex2(ml[0] - M);
  float L = w * ml[1];
  L += dpp<XOR1>(L); L += dpp<XOR2>(L); L += dpp<XOR7>(L);
  p.M = M; p.L = rfl(L);
  p.o = f32x2{0.f, 0.f};
#pragma unroll
  for (int k = 0; k < NW; ++k) { const float wk = rdl(w, k); p.o[0] = fmaf(wk, a[k][0], p.o[0]); p.o[1] = fmaf(wk, a[k][1], p.o[1]); }
  return p;
}
__device__ __forceinline__ void publish(const Part& p, __amdgpu_buffer_rsrc_t rw, unsigned soff, unsigned tagw, int lane) {
  const unsigned a20 = (__float_as_uint(p.o[0]) + 0x800u) >> 12, b20 = (__float_as_uint(p.o[1]) + 0x800u) >> 12;
  const unsigned side = (__float_as_uint(lane < 32 ? p.M : p.L) >> (lane & 31)) & 1u;
  __builtin_amdgcn_raw_buffer_store_b64(u32x2{a20 | (b20 << 20), (b20 >> 12) | (side << 8) | tagw}, rw, lane * 8, soff, 16);
}
template <int N> __device__ __forceinline__ void poll(u32x2 (&gv)[N], __amdgpu_buffer_rsrc_t rw, unsigned soff, int lane) {
  asm volatile("" ::: "memory");
#pragma unroll
  for (int s = 0; s < N; ++s) gv[s] = __builtin_amdgcn_raw_buffer_load_b64(rw, lane * 8 + s * GREC_BYTES, soff, 16);
}
template <int N> __device__ __forceinline__ bool arrived(const u32x2 (&gv)[N], unsigned tagw) {
  unsigned bad = 0;
#pragma unroll
  for (int s = 0; s < N; ++s) bad |= gv[s][1] ^ tagw;
  return !__any(bad >= 512u);
}
template <int N> __device__ __forceinline__ void fold(Part& p, const u32x2 (&gv)[N]) {
  float ms[N], ls[N], Mx = p.M;
#pragma unroll
  for (int s = 0; s < N; ++s) { const u64 sd = __ballot(gv[s][1] & 256u); ms[s] = __uint_as_float((unsigned)sd); ls[s] = __uint_as_float((unsigned)(sd >> 32)); Mx = fmaxf(Mx, ms[s]); }
  const float w0 = ex2(p.M - Mx);
  p.L *= w0; p.o *= w0; p.M = Mx;
#pragma unroll
  for (int s = 0; s < N; ++s) { const float w = ex2(ms[s] - Mx);
    p.L = fmaf(w, ls[s], p.L);
    p.o[0] = fmaf(w, __uint_as_float(gv[s][0] << 12), p.o[0]);
    p.o[1] = fmaf(w, __uint_as_float(__builtin_amdgcn_alignbit(gv[s][1], gv[s][0], 8) & 0xfffff000u), p.o[1]); }
}
__device__ __forceinline__ void signal(int* f, int lane) {
  asm volatile("s_waitcnt lgkmcnt(0)" ::: "memory");
  if (lane == 0) __hip_atomic_fetch_add(f, 1, __ATOMIC_RELAXED, __HIP_MEMORY_SCOPE_WORKGROUP);
}
__device__ __forceinline__ int peek(int* f) {
  const int v = __builtin_amdgcn_readfirstlane(__hip_atomic_load(f, __ATOMIC_RELAXED, __HIP_MEMORY_SCOPE_WORKGROUP));
  asm volatile("" ::: "memory");
  return v;
}
template <int ZZ> __device__ __forceinline__ void await(int* f, int want) {
#pragma nounroll
  while (peek(f) < want) __builtin_amdgcn_s_sleep(ZZ);
}
enum { F_CNT = 0, F_LIST = 1, F_NU = 2, F_HELP = 3, F_NEAR = 4, F_DONE = 5, F_N = 8 };

__global__ void __launch_bounds__(NTHR) attn_decode(const int* __restrict__ bt, const int* __restrict__ ctx, const void* __restrict__ kc, const void* __restrict__ vc,
                                                    const unsigned* __restrict__ q, unsigned char* ws, __bf16* __restrict__ out, unsigned kv_bytes) {
  __shared__ __attribute__((aligned(16))) float s_rec[2 * NW * B * REC];
  __shared__ __attribute__((aligned(16))) u32x2 s_desc[MAXU];
  __shared__ int s_cnt[32], s_flag[F_N];
  const int tid = threadIdx.x, lane = tid & 63, wave16 = __builtin_amdgcn_readfirstlane(tid >> 6), wave = wave16 & (NW - 1);
  const int head = blockIdx.x % H, split = blockIdx.x / H;
  const int oh = head < 28 ? head ^ 2 : head ^ 3;
  const int xn = head < 28 ? XW : XW2;
  const bool giver = xn > 0 && (head < 28 ? (head & 3) == 1 : (head & 1) == 1), taker = xn > 0 && (head < 28 ? (head & 3) == 3 : (head & 1) == 0);
#define STAMP(k)
#define LEAVE()
  STAMP(0);
  if (tid < F_N) s_flag[tid] = 0;
  asm volatile("" :: "s"(bt), "s"(ctx), "s"(kc), "s"(vc), "s"(q), "s"(ws), "s"(out), "s"(kv_bytes));
  __builtin_amdgcn_s_waitcnt(0xc07f);
  __builtin_amdgcn_sched_barrier(0);
  const int gw = split * NW + wave;
  int len[B];
#pragma unroll
  for (int b = 0; b < B; ++b) len[b] = ((cint*)ctx)[b];

  if (wave16 >= NW) {
    const int pg = tid - NW * 64;
    int pb[B];
#pragma unroll
    for (int b = 0; b < B; ++b) pb[b] = bt[b * NB + pg];
    const int b = wave & 3;
    unsigned* epoch_p = reinterpret_cast<unsigned*>(ws + (head * B + b) * WS_LINE);
    const unsigned epoch = __hip_atomic_load(epoch_p, __ATOMIC_RELAXED, __HIP_MEMORY_SCOPE_AGENT);
    const unsigned epoch_o = __hip_atomic_load(reinterpret_cast<unsigned*>(ws + (oh * B + b) * WS_LINE), __ATOMIC_RELAXED, __HIP_MEMORY_SCOPE_AGENT);
    __builtin_amdgcn_sched_barrier(0);
    __builtin_amdgcn_s_barrier();
    asm volatile("" ::: "memory");
    __builtin_amdgcn_sched_barrier(0);
#pragma unroll
    for (int k = 0; k < B; ++k) len[k] = (len[k] <= 0 || len[k] > LK) ? LK : len[k];
    u64 own[B], L[B];
    unsigned msk[B];
#pragma unroll
    for (int k = 0; k < B; ++k) L[k] = __ballot(pg * PG < len[k]);
    const u64 s01 = L[0] & L[1] & __ballot(pb[0] == pb[1]), s02 = L[0] & L[2] & __ballot(pb[0] == pb[2]), s03 = L[0] & L[3] & __ballot(pb[0] == pb[3]);
    const u64 s12 = L[1] & L[2] & __ballot(pb[1] == pb[2]), s13 = L[1] & L[3] & __ballot(pb[1] == pb[3]), s23 = L[2] & L[3] & __ballot(pb[2] == pb[3]);
    own[0] = wave < NSTAT / 64 ? 0ull : L[0];
    own[1] = L[1] & ~s01; own[2] = L[2] & ~(s02 | s12); own[3] = L[3] & ~(s03 | s13 | s23);
#define BIT(m, v) (__builtin_amdgcn_inverse_ballot_w64(m) ? (v) : 0u)
    msk[0] = 1u | BIT(s01, 2u) | BIT(s02, 4u) | BIT(s03, 8u);
    msk[1] = 2u | BIT(s12, 4u) | BIT(s13, 8u);
    msk[2] = 4u | BIT(s23, 8u);
    msk[3] = 8u;
#undef BIT
    const unsigned cn = (unsigned)__popcll(own[0]) | ((unsigned)__popcll(own[1]) << 8) | ((unsigned)__popcll(own[2]) << 16) | ((unsigned)__popcll(own[3]) << 24);
    if (lane < B) s_cnt[lane * NW + wave] = (cn >> (8 * lane)) & 0xffu;
    signal(&s_flag[F_CNT], lane);
    await<1>(&s_flag[F_CNT], NW);
    int incl = s_cnt[lane & 31];
    const int own_cnt = incl;
    incl += __builtin_amdgcn_update_dpp(0, incl, 0x111, 0xf, 0xf, true);
    incl += __builtin_amdgcn_update_dpp(0, incl, 0x112, 0xf, 0xf, true);
    incl += __builtin_amdgcn_update_dpp(0, incl, 0x114, 0xf, 0xf, true);
    incl += __builtin_amdgcn_update_dpp(0, incl, 0x118, 0xf, 0xf, true);
    incl += __builtin_amdgcn_update_dpp(0, incl, 0x142, 0xa, 0xf, false);
    const int excl = incl - own_cnt;
    const int U = __builtin_amdgcn_readlane(incl, 31);
    const int chunk = max(U - EXTRA + NSPLIT / 2, 0) / NSPLIT;
    const int lo = min(split * chunk, U), nu = (split == CONS ? U : min(lo + chunk, U)) - lo;
#pragma unroll
    for (int k = 0; k < B; ++k) {
      const int idx = __builtin_amdgcn_readlane(excl, k * NW + wave) - lo + mbcnt(own[k]);
      if (__builtin_amdgcn_inverse_ballot_w64(own[k]) && (unsigned)idx < (unsigned)nu) s_desc[idx] = u32x2{(unsigned)pb[k] * PAGE_BYTES + head * HEAD_BYTES, (unsigned)(pg * PG) | (msk[k] << 16)};
    }
    if (pg == 0) s_flag[F_NU] = nu;
    signal(&s_flag[F_LIST], lane);
    STAMP(2);
    if (wave >= B || ABL == 3) { LEAVE(); return; }

    const __amdgpu_buffer_rsrc_t rw = __builtin_amdgcn_make_buffer_rsrc(ws, 0, WS_BYTES, 0x00020000);
    const unsigned tag = __builtin_amdgcn_readfirstlane(epoch) + 1u, tagw = tag << 9;
    const unsigned rbase = WS_REC + (head * B + b) * NSLOT * GREC_BYTES;
    if (taker) {
      await<8>(&s_flag[F_HELP], NW);
      publish(merge_waves(s_rec + (NW * B + b) * REC, lane), rw, WS_REC + ((oh * B + b) * NSLOT + NSPLIT + split) * GREC_BYTES, (__builtin_amdgcn_readfirstlane(epoch_o) + 1u) << 9, lane);
    }
    await<16>(&s_flag[F_NEAR], NEARN);
    if (split != CONS) {
      await<1>(&s_flag[F_DONE], NW);
      const Part p = merge_waves(s_rec + b * REC, lane);
      if (ABL < 2 || p.o[0] == 12345.678f) publish(p, rw, rbase + split * GREC_BYTES, tagw, lane);
      STAMP(4);
      LEAVE();
      return;
    }
    Part p; p.o = f32x2{0.f, 0.f}; p.M = M_INIT; p.L = 0.f;
    unsigned spins = 0;
    if (giver) {
      u32x2 gh[NSPLIT];
#pragma nounroll
      do { poll<NSPLIT>(gh, rw, rbase + NSPLIT * GREC_BYTES, lane); } while (!arrived<NSPLIT>(gh, tagw) && ++spins < (1u << 20));
      fold<NSPLIT>(p, gh);
    }
    u32x2 gv[NSPLIT - 1];
    bool merged = false, have = ABL != 0;
#pragma nounroll
    for (;;) {
      if (!have) poll<NSPLIT - 1>(gv, rw, rbase, lane);
      if (!merged && peek(&s_flag[F_DONE]) >= NW) {
        const Part po = merge_waves(s_rec + b * REC, lane);
        const float Mx = fmaxf(p.M, po.M), w0 = ex2(p.M - Mx), w1 = ex2(po.M - Mx);
        p.L = p.L * w0 + po.L * w1; p.o = p.o * w0 + po.o * w1; p.M = Mx;
        merged = true;
      }
      if (!have) have = arrived<NSPLIT - 1>(gv, tagw) || ++spins >= (1u << 20);
      if (have && merged) break;
      __builtin_amdgcn_s_sleep(SLEEP);
    }
    STAMP(4);
    fold<NSPLIT - 1>(p, gv);
    p.o *= __builtin_amdgcn_rcpf(p.L);
    __hip_atomic_store(reinterpret_cast<unsigned*>(out + (b * H + head) * D) + lane, __builtin_bit_cast(unsigned, bf16x2{(__bf16)p.o[0], (__bf16)p.o[1]}), __ATOMIC_RELAXED, __HIP_MEMORY_SCOPE_AGENT);
    if (lane == 0) __hip_atomic_store(epoch_p, tag, __ATOMIC_RELAXED, __HIP_MEMORY_SCOPE_AGENT);
    STAMP(5);
    LEAVE();
    return;
  }

  int2 sp[B];
#pragma unroll
  for (int b = 0; b < B; ++b) { sp[b].x = ((cint*)bt)[b * NB + gw]; sp[b].y = ((cint*)bt)[b * NB + 64 + gw]; }
  __builtin_amdgcn_sched_barrier(0);
  const int r = lane >> 4, c = lane & 15;
  const bool give = giver && wave < xn, take = taker && wave < xn;
  u32x4 qv = *reinterpret_cast<const u32x4*>(q + (((c >> 2) * H + head) * D + 32 * r + 8 * (c & 3)) / 2);
  u32x4 qc = *reinterpret_cast<const u32x4*>(q + (((c >> 2) * H + (take ? oh : head)) * D + 32 * r + 8 * (c & 3)) / 2);
  __builtin_amdgcn_sched_barrier(0);
  __builtin_amdgcn_s_barrier();
  asm volatile("" ::: "memory");
  __builtin_amdgcn_sched_barrier(0);
  unsigned zmask = 0;
#pragma unroll
  for (int b = 0; b < B; ++b) { zmask |= len[b] <= 0 ? (1u << b) : 0u; len[b] = (len[b] <= 0 || len[b] > LK) ? LK : len[b]; }
  const __amdgpu_buffer_rsrc_t rk = __builtin_amdgcn_make_buffer_rsrc(const_cast<void*>(kc), 0, kv_bytes, 0x00020000);
  const __amdgpu_buffer_rsrc_t rv = __builtin_amdgcn_make_buffer_rsrc(const_cast<void*>(vc), 0, kv_bytes, 0x00020000);
  const unsigned voffK = (c >> 2) * TOK_BYTES + r * 64 + (c & 3) * 16;
  const unsigned voffV = r * TOK_BYTES + c * 16;
  u32x4 K[4], V[4];
#define ISSUE(dst, rs, voff, b_) do { \
    _Pragma("unroll") for (int t_ = 0; t_ < 4; ++t_) { dst[t_] = __builtin_amdgcn_raw_buffer_load_b128(rs, voff, (b_) + t_ * 4 * TOK_BYTES, 2); __builtin_amdgcn_sched_barrier(0); } } while (0)
  const bool slive0 = gw * PG < len[0], slive1 = (64 + gw) * PG < len[0];
  const unsigned sb0 = slive0 ? (unsigned)sp[0].x * PAGE_BYTES + head * HEAD_BYTES : kv_bytes;
  const unsigned sb1 = slive1 ? (unsigned)sp[0].y * PAGE_BYTES + head * HEAD_BYTES : kv_bytes;
  const unsigned b0 = take ? (slive1 ? (unsigned)sp[0].y * PAGE_BYTES + oh * HEAD_BYTES : kv_bytes) : sb0;
  STAMP(1);
  ISSUE(K, rk, voffK, b0);
  ISSUE(V, rv, voffV, b0);
  unsigned sm0 = slive0 ? 1u : 0u, sm1 = slive1 ? 1u : 0u;
#pragma unroll
  for (int b = 1; b < B; ++b) {
    sm0 |= (slive0 && gw * PG < len[b] && sp[b].x == sp[0].x) ? (1u << b) : 0u;
    sm1 |= (slive1 && (64 + gw) * PG < len[b] && sp[b].y == sp[0].y) ? (1u << b) : 0u;
  }
  sm0 = (unsigned)(gw * PG) | (sm0 << 16); sm1 = (unsigned)((64 + gw) * PG) | (sm1 << 16);
  const int sb = c >> 2, su = c & 3;
  const int lim = (sb == 0 ? len[0] : sb == 1 ? len[1] : sb == 2 ? len[2] : len[3]) - (4 * su + r);
  const unsigned sbit = 0x10000u << sb;
  const unsigned mA = su == 0 ? 0x0000ffffu : su == 1 ? 0xffff0000u : 0u, mB = su == 2 ? 0x0000ffffu : su == 3 ? 0xffff0000u : 0u;
  float m = M_INIT, l = 0.f;
  f32x4 acc0 = f32x4{0.f, 0.f, 0.f, 0.f}, acc1 = acc0;
#define FLUSH(kind) do { float lt = l; lt += dpp<XOR1>(lt); lt += dpp<XOR2>(lt); lt = xrow_sum(lt); \
    float* rec = s_rec + (((kind) * NW + wave) * B + r) * REC; \
    *reinterpret_cast<f32x4*>(rec + 4 + 8 * c) = acc0; *reinterpret_cast<f32x4*>(rec + 8 + 8 * c) = acc1; \
    if (lane < 16 && su == 0) *reinterpret_cast<f32x2*>(s_rec + (((kind) * NW + wave) * B + sb) * REC) = f32x2{m, lt}; } while (0)
  if (taker && !take) { FLUSH(1); signal(&s_flag[F_HELP], lane); }
  if ((zmask >> sb) & 1u) { qv = u32x4{0u, 0u, 0u, 0u}; qc = qv; }
  await<1>(&s_flag[F_LIST], NW);
  const int nu = peek(&s_flag[F_NU]);
  const int np = give ? 1 : take ? 3 : 2;
  const int wv = taker ? (wave - xn) & (NW - 1) : wave;
  const int n = np + (nu > wv ? (nu - wv + NW - 1) / NW : 0);
  const int sl = take ? lane - 1 : lane;
  unsigned dP = sl == 0 ? sb0 : sl == 1 ? sb1 : kv_bytes, dM = sl == 0 ? sm0 : sl == 1 ? sm1 : sm1 | 0x80000000u;
  if (lane >= n) dP = kv_bytes;
  if (lane >= np && lane < n) { const u32x2 dsc = s_desc[wv + NW * (lane - np)]; dP = dsc[0]; dM = dsc[1]; }
  STAMP(2);

  int i = 0;
  do {
    const unsigned dm = __builtin_amdgcn_readlane(dM, i), nb = __builtin_amdgcn_readlane(dP, i + 1);
    if (i + 1 == n && lane == 0) __hip_atomic_fetch_add(&s_flag[F_NEAR], 1, __ATOMIC_RELAXED, __HIP_MEMORY_SCOPE_WORKGROUP);
    f32x4 S[4];
#pragma unroll
    for (int t = 0; t < 4; ++t) S[t] = __builtin_amdgcn_mfma_f32_16x16x32_bf16(__builtin_bit_cast(bf16x8, K[t]), __builtin_bit_cast(bf16x8, qc), f32x4{0.f, 0.f, 0.f, 0.f}, 0, 0, 0);
#pragma unroll
    for (int t = 0; t < 4; ++t) asm volatile("" : "+v"(S[t]));
    __builtin_amdgcn_sched_barrier(0);
    ISSUE(K, rk, voffK, nb);
    float y[4];
#pragma unroll
    for (int t = 0; t < 4; ++t) y[t] = (dpp<Q0>(S[t][0]) + dpp<Q1>(S[t][1])) + (dpp<Q2>(S[t][2]) + dpp<Q3>(S[t][3]));
    const float x = (su & 2) ? ((su & 1) ? y[3] : y[2]) : ((su & 1) ? y[1] : y[0]);
    const bool live = (dm & sbit) != 0u && (int)(dm & 0xffffu) < lim;
    const float s = live ? x * C2 : -INFINITY;
    float pm = fmaxf(s, dpp<XOR1>(s)); pm = fmaxf(pm, dpp<XOR2>(pm)); pm = xrow_max(pm);
    const float mn = fmaxf(m, pm);
    const float p = ex2(s - mn);
    if (__any(mn > m)) {
      const float al = ex2(m - mn);
      l *= al; m = mn;
      int ar = __builtin_amdgcn_update_dpp(0, __builtin_bit_cast(int, al), BCAST + 0, 0x1, 0xf, false);
      ar = __builtin_amdgcn_update_dpp(ar, __builtin_bit_cast(int, al), BCAST + 4, 0x2, 0xf, false);
      ar = __builtin_amdgcn_update_dpp(ar, __builtin_bit_cast(int, al), BCAST + 8, 0x4, 0xf, false);
      ar = __builtin_amdgcn_update_dpp(ar, __builtin_bit_cast(int, al), BCAST + 12, 0x8, 0xf, false);
      const float a = __builtin_bit_cast(float, ar);
      acc0 *= a; acc1 *= a;
    }
    const unsigned pk = __builtin_bit_cast(unsigned, bf16x2{(__bf16)p, (__bf16)p});
    l += __uint_as_float(pk & 0xffff0000u);
#pragma unroll
    for (int t = 0; t < 4; ++t) {
      const unsigned pt = t == 0 ? dppu<Q0>(pk) : t == 1 ? dppu<Q1>(pk) : t == 2 ? dppu<Q2>(pk) : dppu<Q3>(pk);
      const s16x4 a = __builtin_bit_cast(s16x4, u32x2{pt & mA, pt & mB});
      acc0 = __builtin_amdgcn_mfma_f32_16x16x16bf16_1k(a, __builtin_bit_cast(s16x4, u32x2{V[t][0], V[t][1]}), acc0, 0, 0, 0);
      acc1 = __builtin_amdgcn_mfma_f32_16x16x16bf16_1k(a, __builtin_bit_cast(s16x4, u32x2{V[t][2], V[t][3]}), acc1, 0, 0, 0);
    }
    asm volatile("" : "+v"(acc0), "+v"(acc1));
    __builtin_amdgcn_sched_barrier(0);
    ISSUE(V, rv, voffV, nb);
    if ((int)dm < 0) {
      FLUSH(1);
      signal(&s_flag[F_HELP], lane);
      m = M_INIT; l = 0.f; acc0 = f32x4{0.f, 0.f, 0.f, 0.f}; acc1 = acc0; qc = qv;
    }
  } while (++i < n);
#undef ISSUE
  if (ABL == 3) { if (acc0[0] + acc1[1] + l + m == 12345.678f) out[0] = (__bf16)1.f; return; }
  FLUSH(0);
#undef FLUSH
  signal(&s_flag[F_DONE], lane);
  STAMP(3);
  LEAVE();
}
}

extern "C" void kernel_launch(void* const* d_in, const int* in_sizes, int n_in, void* d_out, int out_size, void* d_ws, size_t ws_size, hipStream_t stream) {
  const unsigned kv_bytes = (unsigned)in_sizes[1] * 2u;
  attn_decode<<<H * NSPLIT, NTHR, 0, stream>>>((const int*)d_in[3], (const int*)d_in[4], d_in[1], d_in[2], (const unsigned*)d_in[0], (unsigned char*)d_ws, (__bf16*)d_out, kv_bytes);
}
```

```cpp
#include <hip/hip_runtime.h>
#include <hip/hip_bf16.h>
#include <cstdint>

#define HSHIFT 1
#define EXTRA -6
#define SLEEP 4
#define ABL 0

namespace {
constexpr int B = 4, H = 32, D = 128, PG = 16, NB = 512, LK = NB * PG;
constexpr int NSPLIT = 8, NW = 8, NTHR = NW * 64;
constexpr unsigned PAGE_BYTES = PG * H * D * 2, TOK_BYTES = H * D * 2, HEAD_BYTES = D * 2;
constexpr float C2 = 0.088388347648318440f * 1.4426950408889634f;
constexpr float M_INIT = -1e30f;
constexpr int NSTAT = NSPLIT * NW;
constexpr int MAXU = (B * NB) / NSPLIT + 16;
constexpr int REC = 132;
constexpr int CONS = NSPLIT - 1;
constexpr unsigned WS_LINE = 128, WS_REC = 65536, GREC_BYTES = 512;
constexpr unsigned WS_BYTES = WS_REC + H * B * NSPLIT * GREC_BYTES;
static_assert(NTHR == NB && NSTAT == 64 && NW >= B, "one thread per logical page in the prologue; wave b finishes sequence b");

typedef unsigned u32x4 __attribute__((ext_vector_type(4)));
typedef unsigned u32x2 __attribute__((ext_vector_type(2)));
typedef float f32x2 __attribute__((ext_vector_type(2)));
typedef float f32x4 __attribute__((ext_vector_type(4)));
typedef __bf16 bf16x2 __attribute__((ext_vector_type(2)));
typedef __bf16 bf16x8 __attribute__((ext_vector_type(8)));
typedef short s16x4 __attribute__((ext_vector_type(4)));
typedef unsigned long long u64;

template <int CTRL> __device__ __forceinline__ float dpp(float x) {
  return __builtin_bit_cast(float, __builtin_amdgcn_mov_dpp(__builtin_bit_cast(int, x), CTRL, 0xf, 0xf, true));
}
template <int CTRL> __device__ __forceinline__ unsigned dppu(unsigned x) { return (unsigned)__builtin_amdgcn_mov_dpp((int)x, CTRL, 0xf, 0xf, true); }
constexpr int XOR1 = 0xB1, XOR2 = 0x4E, XOR7 = 0x141, BCAST = 0x150, Q0 = 0x00, Q1 = 0x55, Q2 = 0xAA, Q3 = 0xFF;
__device__ __forceinline__ float ex2(float x) { return __builtin_amdgcn_exp2f(x); }
__device__ __forceinline__ float max3(float a, float b, float c) { return fmaxf(fmaxf(a, b), c); }
__device__ __forceinline__ float xrow_sum(float x) {
  auto s = __builtin_amdgcn_permlane16_swap(__float_as_uint(x), __float_as_uint(x), false, false);
  x = __uint_as_float(s[0]) + __uint_as_float(s[1]);
  auto t = __builtin_amdgcn_permlane32_swap(__float_as_uint(x), __float_as_uint(x), false, false);
  return __uint_as_float(t[0]) + __uint_as_float(t[1]);
}
__device__ __forceinline__ float rdl(float x, int l) { return __builtin_bit_cast(float, __builtin_amdgcn_readlane(__builtin_bit_cast(int, x), l)); }
__device__ __forceinline__ float rfl(float x) { return __builtin_bit_cast(float, __builtin_amdgcn_readfirstlane(__builtin_bit_cast(int, x))); }
__device__ __forceinline__ int mbcnt(u64 m) { return (int)__builtin_amdgcn_mbcnt_hi((unsigned)(m >> 32), __builtin_amdgcn_mbcnt_lo((unsigned)m, 0u)); }

__global__ void __launch_bounds__(NTHR) attn_decode(const int* __restrict__ bt, const int* __restrict__ ctx, const void* __restrict__ kc, const void* __restrict__ vc,
                                                    const unsigned* __restrict__ q, unsigned char* ws, __bf16* __restrict__ out, unsigned kv_bytes) {
  __shared__ __attribute__((aligned(16))) float s_rec[NW * B * REC];
  __shared__ __attribute__((aligned(16))) u32x2 s_desc[MAXU + NSTAT];
  __shared__ int s_cnt[32];
  const int tid = threadIdx.x, lane = tid & 63, wave = __builtin_amdgcn_readfirstlane(tid >> 6);
  const int r = lane >> 4, c = lane & 15, sb = c >> 2, su = c & 3;
  const int head = (blockIdx.x + HSHIFT) % H, split = blockIdx.x / H;

  const int gw = split * NW + wave;
  const int sp0 = bt[gw];
  unsigned lm1[B];
#pragma unroll
  for (int b = 0; b < B; ++b) lm1[b] = (unsigned)ctx[b] - 1u;
  int pb[B];
#pragma unroll
  for (int b = 0; b < B; ++b) pb[b] = bt[b * NB + tid];
  const u32x4 qv = *reinterpret_cast<const u32x4*>(q + ((sb * H + head) * D + 32 * r + 8 * su) / 2);
  const int lenv = ctx[sb];
  unsigned* epoch_p = reinterpret_cast<unsigned*>(ws + (head * B + (wave & 3)) * WS_LINE);
  const unsigned epoch = __hip_atomic_load(epoch_p, __ATOMIC_RELAXED, __HIP_MEMORY_SCOPE_AGENT);
  const __amdgpu_buffer_rsrc_t rk = __builtin_amdgcn_make_buffer_rsrc(const_cast<void*>(kc), 0, kv_bytes, 0x00020000);
  const __amdgpu_buffer_rsrc_t rv = __builtin_amdgcn_make_buffer_rsrc(const_cast<void*>(vc), 0, kv_bytes, 0x00020000);
  const unsigned voffK = sb * TOK_BYTES + head * HEAD_BYTES + r * 64 + su * 16;
  const unsigned voffV = r * TOK_BYTES + head * HEAD_BYTES + c * 16;
  __builtin_amdgcn_sched_barrier(0);
  u32x4 K[4], V[4];
#define ISSUE(dst, rs, voff, b_) do { \
    _Pragma("unroll") for (int t_ = 0; t_ < 4; ++t_) { dst[t_] = __builtin_amdgcn_raw_buffer_load_b128(rs, voff, (b_) + t_ * 4 * TOK_BYTES, 2); __builtin_amdgcn_sched_barrier(0); } } while (0)
  const unsigned b0 = (unsigned)(gw * PG) <= lm1[0] ? (unsigned)sp0 * PAGE_BYTES : kv_bytes;
  ISSUE(K, rk, voffK, b0);

  u64 own[B], L[B];
#pragma unroll
  for (int b = 0; b < B; ++b) L[b] = __ballot((unsigned)(tid * PG) <= lm1[b]);
  const u64 s01 = L[0] & L[1] & __ballot(pb[0] == pb[1]), s02 = L[0] & L[2] & __ballot(pb[0] == pb[2]), s03 = L[0] & L[3] & __ballot(pb[0] == pb[3]);
  const u64 s12 = L[1] & L[2] & __ballot(pb[1] == pb[2]), s13 = L[1] & L[3] & __ballot(pb[1] == pb[3]), s23 = L[2] & L[3] & __ballot(pb[2] == pb[3]);
  own[0] = wave == 0 ? 0ull : L[0];
  own[1] = L[1] & ~s01; own[2] = L[2] & ~(s02 | s12); own[3] = L[3] & ~(s03 | s13 | s23);
  {
    const unsigned cn = (unsigned)__popcll(own[0]) | ((unsigned)__popcll(own[1]) << 8) | ((unsigned)__popcll(own[2]) << 16) | ((unsigned)__popcll(own[3]) << 24);
    if (lane < B) s_cnt[lane * NW + wave] = (cn >> (8 * lane)) & 0xffu;
  }
  __syncthreads();
  int incl = s_cnt[lane & 31];
  const int own_cnt = incl;
  incl += __builtin_amdgcn_update_dpp(0, incl, 0x111, 0xf, 0xf, true);
  incl += __builtin_amdgcn_update_dpp(0, incl, 0x112, 0xf, 0xf, true);
  incl += __builtin_amdgcn_update_dpp(0, incl, 0x114, 0xf, 0xf, true);
  incl += __builtin_amdgcn_update_dpp(0, incl, 0x118, 0xf, 0xf, true);
  incl += __builtin_amdgcn_update_dpp(0, incl, 0x142, 0xa, 0xf, false);
  const int U = __builtin_amdgcn_readlane(incl, 31);
  const int chunk = (U - EXTRA + NSPLIT / 2) / NSPLIT;
  const int lo = min(split * chunk, U), nu = (split == CONS ? U : min(lo + chunk, U)) - lo;
  const int excl = incl - own_cnt - lo;
#define BIT(m, v) (__builtin_amdgcn_inverse_ballot_w64(m) ? (v) : 0u)
  unsigned dw[B];
  dw[0] = (unsigned)(tid * PG) | BIT(L[0], 1u) | BIT(s01, 2u) | BIT(s02, 4u) | BIT(s03, 8u);
  dw[1] = (unsigned)(tid * PG) | 2u | BIT(s12, 4u) | BIT(s13, 8u);
  dw[2] = (unsigned)(tid * PG) | 4u | BIT(s23, 8u);
  dw[3] = (unsigned)(tid * PG) | 8u;
#undef BIT
#pragma unroll
  for (int b = 0; b < B; ++b) {
    int idx = __builtin_amdgcn_readlane(excl, b * NW + wave) + mbcnt(own[b]);
    bool put = __builtin_amdgcn_inverse_ballot_w64(own[b]) && (unsigned)idx < (unsigned)nu;
    if (b == 0 && wave == 0) { idx = MAXU + lane; put = true; }
    if (put) s_desc[idx] = u32x2{(unsigned)pb[b] * PAGE_BYTES, dw[b]};
  }
  ISSUE(V, rv, voffV, b0);
  __syncthreads();
  const int n = 1 + ((nu - wave + NW - 1) >> 3);
  u32x2 dsc = s_desc[lane == 0 ? MAXU + gw : min(wave + NW * (lane - 1), MAXU)];
  const unsigned dP = lane < n ? dsc[0] : kv_bytes, dM = dsc[1];

  const int lim = (int)min((unsigned)lenv - 1u, (unsigned)LK) - (4 * su + r);
  const float cs = lenv > 0 ? C2 : 0.f;
  const unsigned sbit = 1u << sb;
  const unsigned mA = su < 2 ? 0xffffu << (16 * su) : 0u, mB = su < 2 ? 0u : 0xffffu << (16 * (su - 2));
  float m = M_INIT, l = 0.f;
  f32x4 acc0 = f32x4{0.f, 0.f, 0.f, 0.f}, acc1 = acc0;
  int i = 0;
  do {
    const unsigned dm = __builtin_amdgcn_readlane(dM, i), nb = __builtin_amdgcn_readlane(dP, i + 1);
    f32x4 S[4];
#pragma unroll
    for (int t = 0; t < 4; ++t) S[t] = __builtin_amdgcn_mfma_f32_16x16x32_bf16(__builtin_bit_cast(bf16x8, K[t]), __builtin_bit_cast(bf16x8, qv), f32x4{0.f, 0.f, 0.f, 0.f}, 0, 0, 0);
#pragma unroll
    for (int t = 0; t < 4; ++t) asm volatile("" : "+v"(S[t]));
    __builtin_amdgcn_sched_barrier(0);
    ISSUE(K, rk, voffK, nb);
    float y[4];
#pragma unroll
    for (int t = 0; t < 4; ++t) y[t] = ((dpp<Q0>(S[t][0]) + dpp<Q1>(S[t][1])) + dpp<Q2>(S[t][2])) + dpp<Q3>(S[t][3]);
    const float x = (su & 2) ? ((su & 1) ? y[3] : y[2]) : ((su & 1) ? y[1] : y[0]);
    const bool live = (dm & sbit) != 0u && (int)(dm & ~15u) <= lim;
    const float s = live ? x * cs : -INFINITY;
    float mn = max3(m, s, dpp<XOR1>(s));
    mn = max3(mn, mn, dpp<XOR2>(mn));
    { auto e = __builtin_amdgcn_permlane16_swap(__float_as_uint(mn), __float_as_uint(mn), false, false);
      mn = max3(m, __uint_as_float(e[0]), __uint_as_float(e[1]));
      auto f = __builtin_amdgcn_permlane32_swap(__float_as_uint(mn), __float_as_uint(mn), false, false);
      mn = max3(m, __uint_as_float(f[0]), __uint_as_float(f[1])); }
    const float p = ex2(s - mn);
    if (__any(mn > m)) {
      const float al = ex2(m - mn);
      l *= al; m = mn;
      int ar = __builtin_amdgcn_update_dpp(0, __builtin_bit_cast(int, al), BCAST + 0, 0x1, 0xf, false);
      ar = __builtin_amdgcn_update_dpp(ar, __builtin_bit_cast(int, al), BCAST + 4, 0x2, 0xf, false);
      ar = __builtin_amdgcn_update_dpp(ar, __builtin_bit_cast(int, al), BCAST + 8, 0x4, 0xf, false);
      ar = __builtin_amdgcn_update_dpp(ar, __builtin_bit_cast(int, al), BCAST + 12, 0x8, 0xf, false);
      const float a = __builtin_bit_cast(float, ar);
      acc0 *= a; acc1 *= a;
    }
    const unsigned pk = __builtin_bit_cast(unsigned, bf16x2{(__bf16)p, (__bf16)p});
    l += __uint_as_float(pk & 0xffff0000u);
#pragma unroll
    for (int t = 0; t < 4; ++t) {
      const unsigned pt = t == 0 ? dppu<Q0>(pk) : t == 1 ? dppu<Q1>(pk) : t == 2 ? dppu<Q2>(pk) : dppu<Q3>(pk);
      const s16x4 a = __builtin_bit_cast(s16x4, u32x2{pt & mA, pt & mB});
      acc0 = __builtin_amdgcn_mfma_f32_16x16x16bf16_1k(a, __builtin_bit_cast(s16x4, u32x2{V[t][0], V[t][1]}), acc0, 0, 0, 0);
      acc1 = __builtin_amdgcn_mfma_f32_16x16x16bf16_1k(a, __builtin_bit_cast(s16x4, u32x2{V[t][2], V[t][3]}), acc1, 0, 0, 0);
    }
    asm volatile("" : "+v"(acc0), "+v"(acc1));
    __builtin_amdgcn_sched_barrier(0);
    ISSUE(V, rv, voffV, nb);
    ++i;
  } while (i < n);
#undef ISSUE
  if (ABL == 3) { if (acc0[0] + acc1[1] + l + m == 12345.678f) out[0] = (__bf16)1.f; return; }
  {
    l += dpp<XOR1>(l); l += dpp<XOR2>(l); l = xrow_sum(l);
    float* rec = s_rec + (wave * B + r) * REC;
    *reinterpret_cast<f32x4*>(rec + 4 + 8 * c) = acc0;
    *reinterpret_cast<f32x4*>(rec + 8 + 8 * c) = acc1;
    if (c == 4 * r) *reinterpret_cast<f32x2*>(rec) = f32x2{m, l};
  }
  __syncthreads();
  if (wave >= B) return;

  const float* rb = s_rec + wave * REC;
  const f32x2 ml = *reinterpret_cast<const f32x2*>(rb + (lane & 7) * B * REC);
  f32x2 a[NW];
#pragma unroll
  for (int k = 0; k < NW; ++k) a[k] = *reinterpret_cast<const f32x2*>(rb + k * B * REC + 4 + 2 * lane);
  float M = max3(ml[0], ml[0], dpp<XOR1>(ml[0]));
  M = max3(M, M, dpp<XOR2>(M)); M = max3(M, M, dpp<XOR7>(M));
  const float w = ex2(ml[0] - M);
  float Ls = w * ml[1];
  Ls += dpp<XOR1>(Ls); Ls += dpp<XOR2>(Ls); Ls += dpp<XOR7>(Ls);
  Ls = rfl(Ls);
  f32x2 o = f32x2{0.f, 0.f};
#pragma unroll
  for (int k = 0; k < NW; ++k) { const float wk = rdl(w, k); o = __builtin_elementwise_fma(f32x2{wk, wk}, a[k], o); }

  const unsigned tag = __builtin_amdgcn_readfirstlane(epoch) + 1u, tagw = tag << 9;
  const __amdgpu_buffer_rsrc_t rw = __builtin_amdgcn_make_buffer_rsrc(ws, 0, WS_BYTES, 0x00020000);
  const unsigned rbase = WS_REC + (head * B + wave) * NSPLIT * GREC_BYTES;
  if (split != CONS) {
    const unsigned a20 = (__float_as_uint(o[0]) + 0x800u) >> 12, b20 = (__float_as_uint(o[1]) + 0x800u) >> 12;
    const unsigned side = (__float_as_uint(lane < 32 ? M : Ls) >> (lane & 31)) & 1u;
    __builtin_amdgcn_raw_buffer_store_b64(u32x2{a20 | (b20 << 20), (b20 >> 12) | (side << 8) | tagw}, rw, lane * 8, rbase + split * GREC_BYTES, 16);
    return;
  }
  u32x2 gv[NSPLIT - 1];
  unsigned spins = 0;
#pragma nounroll
  for (;;) {
    asm volatile("" ::: "memory");
    unsigned bad = 0;
#pragma unroll
    for (int s = 0; s < NSPLIT - 1; ++s) gv[s] = __builtin_amdgcn_raw_buffer_load_b64(rw, lane * 8 + s * GREC_BYTES, rbase, 16);
#pragma unroll
    for (int s = 0; s < NSPLIT - 1; ++s) bad |= gv[s][1] ^ tagw;
    if (ABL || !__any(bad >= 512u) || ++spins > (1u << 20)) break;
    __builtin_amdgcn_s_sleep(SLEEP);
  }
  float ms[NSPLIT - 1], ls[NSPLIT - 1], Mx = M;
#pragma unroll
  for (int s = 0; s < NSPLIT - 1; ++s) { const u64 sd = __ballot(gv[s][1] & 256u); ms[s] = __uint_as_float((unsigned)sd); ls[s] = __uint_as_float((unsigned)(sd >> 32)); Mx = fmaxf(Mx, ms[s]); }
  const float w0 = ex2(M - Mx);
  Ls *= w0; o *= w0;
#pragma unroll
  for (int s = 0; s < NSPLIT - 1; ++s) { const float ws_ = ex2(ms[s] - Mx);
    Ls = fmaf(ws_, ls[s], Ls);
    o[0] = fmaf(ws_, __uint_as_float(gv[s][0] << 12), o[0]);
    o[1] = fmaf(ws_, __uint_as_float(__builtin_amdgcn_alignbit(gv[s][1], gv[s][0], 8) & 0xfffff000u), o[1]); }
  o *= __builtin_amdgcn_rcpf(Ls);
  __hip_atomic_store(reinterpret_cast<unsigned*>(out + (wave * H + head) * D) + lane, __builtin_bit_cast(unsigned, bf16x2{(__bf16)o[0], (__bf16)o[1]}), __ATOMIC_RELAXED, __HIP_MEMORY_SCOPE_AGENT);
  if (lane == 0) __hip_atomic_store(epoch_p, tag, __ATOMIC_RELAXED, __HIP_MEMORY_SCOPE_AGENT);
}
}

extern "C" void kernel_launch(void* const* d_in, const int* in_sizes, int n_in, void* d_out, int out_size, void* d_ws, size_t ws_size, hipStream_t stream) {
  const unsigned kv_bytes = (unsigned)in_sizes[1] * 2u;
  attn_decode<<<H * NSPLIT, NTHR, 0, stream>>>((const int*)d_in[3], (const int*)d_in[4], d_in[1], d_in[2], (const unsigned*)d_in[0], (unsigned char*)d_ws, (__bf16*)d_out, kv_bytes);
}
```
